```python
import torch

def _softmax(x, axis=-1):
    return torch.softmax(x, dim=axis)
def _gelu(x):
    return torch.nn.functional.gelu(x, approximate="tanh")
def _silu(x):
    return torch.nn.functional.silu(x)
def _sigmoid(x):
    return torch.sigmoid(x)
def _layernorm(x, axis=-1, eps=1e-5):
    m = x.mean(dim=axis, keepdim=True)
    v = x.var(dim=axis, keepdim=True, unbiased=False)
    return (x - m) / torch.sqrt(v + eps)
def _scatter_add(tgt, idx, src, axis=0):
    return tgt.clone().index_add_(axis, idx, src)
def _segment_sum(src, seg, n):
    out = torch.zeros((n,) + src.shape[1:], dtype=src.dtype)
    return out.index_add_(0, seg, src)
def _pad(x, pw):
    flat = tuple(v for pair in reversed(pw) for v in pair)
    return torch.nn.functional.pad(x, flat)

kernel_name = 'gated_mix__mha_block__ssm_conv_s4d__prompt'

def setup_inputs():

    _w = (lambda a: (lambda t: t.to(torch.bfloat16) if t.is_floating_point() else t)(torch.as_tensor(a)))
    def _A_setup():
        B, S, D, H = 1, 2048, 576, 32
        return {
            "x": _w((lambda *s: torch.randn(*s, dtype=torch.bfloat16) * 0.05)(B, S, D)),
            "wqkv": _w((lambda *s: torch.randn(*s, dtype=torch.bfloat16) * 0.8)(D, 3 * D)),
            "wo": _w((lambda *s: torch.randn(*s, dtype=torch.bfloat16) * 0.05)(D, D)),
        }
    _A_in = _A_setup()

    def _B_setup():
        Bsz, L, D, N = 1, 2048, 576, 128
        return {
            "u": _w((lambda *s: torch.randn(*s, dtype=torch.bfloat16) * 0.05)(Bsz, L, D)),
            "log_A_real": _w((lambda *s: torch.randn(*s, dtype=torch.bfloat16) * 6.4)(D, N) * 0.5),
            "A_imag": _w((lambda *s: torch.randn(*s, dtype=torch.bfloat16) * 0.1)(D, N) * 3.14),
            "C_real": _w((lambda *s: torch.randn(*s, dtype=torch.bfloat16) * 0.4)(D, N) / N ** 0.5),
            "C_imag": _w((lambda *s: torch.randn(*s, dtype=torch.bfloat16) * 0.4)(D, N) / N ** 0.5),
            "log_dt": _w((lambda *s: torch.randn(*s, dtype=torch.bfloat16) * 0.1)(D) * 0.5 - 4.0),
            "D_skip": _w((lambda *s: torch.randn(*s, dtype=torch.bfloat16) * 0.1)(D)),
        }
    _B_in = _B_setup()

    return {
        "A_x": _A_in["x"],
        "A_wqkv": _A_in["wqkv"],
        "A_wo": _A_in["wo"],
        "B_u": _B_in["u"],
        "B_log_A_real": _B_in["log_A_real"],
        "B_A_imag": _B_in["A_imag"],
        "B_C_real": _B_in["C_real"],
        "B_C_imag": _B_in["C_imag"],
        "B_log_dt": _B_in["log_dt"],
        "B_D_skip": _B_in["D_skip"],
        "gate_w": _w((lambda *s: torch.randn(*s, dtype=torch.bfloat16) * 0.05)(576) * 16.0),
    }

def reference(A_x, A_wqkv, A_wo, B_u, B_log_A_real, B_A_imag, B_C_real, B_C_imag, B_log_dt, B_D_skip, gate_w):
    def _A_ref(x, wqkv, wo):
        B, S, D = x.shape
        H = 32
        Dh = D // H
        qkv = (lambda x, *d: x.permute(*d))((x @ wqkv).reshape(B, S, 3, H, Dh), 2, 0, 3, 1, 4)
        q, k, v = qkv[0], qkv[1], qkv[2]
        scores = torch.einsum("bhqd,bhkd->bhqk", q, k) * (Dh ** -0.5)
        p = _softmax(scores, axis=-1)
        o = torch.einsum("bhqk,bhkd->bhqd", p, v)
        return (lambda x, *d: x.permute(*d))(o, 0, 2, 1, 3).reshape(B, S, D) @ wo

    def _B_ref(u, log_A_real, A_imag, C_real, C_imag, log_dt, D_skip):
        L = u.shape[1]
        dt = torch.exp(log_dt)
        A = -torch.exp(log_A_real) + 1j * A_imag
        C = (C_real + 1j * C_imag) * dt[:, None]
        pos = (lambda x, dt: x.to(dt))(torch.arange(L), log_dt.dtype)
        P = torch.exp((A * dt[:, None])[:, :, None] * pos[None, None, :])
        K = 2.0 * torch.einsum("dn,dnl->dl", C, P).real
        L2 = 2 * L
        u_t = (lambda x, *d: x.permute(*d))(u, 0, 2, 1)
        Kf = (lambda x, *a, **k: torch.fft.rfft(x.float(), *a, **k))(K, L2)
        Uf = (lambda x, *a, **k: torch.fft.rfft(x.float(), *a, **k))(u_t, L2)
        y = (lambda x, *a, **k: torch.fft.irfft(x, *a, **k).to(torch.bfloat16))(Uf * Kf[None, :, :], L2)[:, :, :L]
        y = (lambda x, *d: x.permute(*d))(y, 0, 2, 1)
        return y + u * D_skip[None, None, :]

    x = A_x
    g = _sigmoid((x * gate_w).sum(-1, keepdims=True))
    a = _A_ref(x, A_wqkv, A_wo)
    b = _B_ref(x, B_log_A_real, B_A_imag, B_C_real, B_C_imag, B_log_dt, B_D_skip)
    return g * a + (1 - g) * b
```

```cpp
#include <hip/hip_runtime.h>
#include <hip/hip_bf16.h>

typedef __hip_bfloat16 bf16;
constexpr int S = 2048, D = 576, H = 32, DH = 18, N = 128;

__device__ __forceinline__ float b2f(bf16 v) { return __bfloat162float(v); }
__device__ __forceinline__ bf16 f2b(float v) { return __float2bfloat16(v); }
__device__ __forceinline__ float rb(float v) { return __bfloat162float(__float2bfloat16(v)); }

__global__ void gemm_naive(const bf16* A, const bf16* B, bf16* C, int M, int Nn, int K) {
  int col = blockIdx.x * blockDim.x + threadIdx.x;
  int row = blockIdx.y;
  if (col >= Nn) return;
  float acc = 0.f;
  for (int k = 0; k < K; ++k) acc += b2f(A[row * K + k]) * b2f(B[k * Nn + col]);
  C[row * Nn + col] = f2b(acc);
}

__global__ void attn_naive(const bf16* qkv, bf16* o) {
  int h = blockIdx.x, q = blockIdx.y, t = threadIdx.x;
  __shared__ float sc[S];
  __shared__ float red[256];
  const bf16* qp = qkv + q * 3 * D + h * DH;
  float mx = -1e30f;
  for (int k = t; k < S; k += 256) {
    const bf16* kp = qkv + k * 3 * D + D + h * DH;
    float s = 0.f;
    for (int d = 0; d < DH; ++d) s += b2f(qp[d]) * b2f(kp[d]);
    s = rb(s);
    s = rb(s * rb(0.23570226039551584f));
    sc[k] = s;
    mx = fmaxf(mx, s);
  }
  red[t] = mx; __syncthreads();
  for (int o2 = 128; o2 > 0; o2 >>= 1) { if (t < o2) red[t] = fmaxf(red[t], red[t + o2]); __syncthreads(); }
  mx = red[0]; __syncthreads();
  float sum = 0.f;
  for (int k = t; k < S; k += 256) { float e = expf(sc[k] - mx); sc[k] = e; sum += e; }
  red[t] = sum; __syncthreads();
  for (int o2 = 128; o2 > 0; o2 >>= 1) { if (t < o2) red[t] += red[t + o2]; __syncthreads(); }
  sum = red[0]; __syncthreads();
  for (int k = t; k < S; k += 256) sc[k] = rb(sc[k] / sum);
  __syncthreads();
  if (t < DH) {
    float acc = 0.f;
    for (int k = 0; k < S; ++k) acc += sc[k] * b2f(qkv[k * 3 * D + 2 * D + h * DH + t]);
    o[q * D + h * DH + t] = f2b(acc);
  }
}

__global__ void kgen_naive(const bf16* lar, const bf16* aim, const bf16* cr, const bf16* ci, const bf16* ldt, float* Kout) {
  int d = blockIdx.y;
  int l = blockIdx.x * blockDim.x + threadIdx.x;
  float dt = rb(expf(b2f(ldt[d])));
  float pos = rb((float)l);
  float acc = 0.f;
  for (int n = 0; n < N; ++n) {
    float ar = -rb(expf(b2f(lar[d * N + n])));
    float ai = b2f(aim[d * N + n]);
    float Cr = b2f(cr[d * N + n]) * dt, Ci = b2f(ci[d * N + n]) * dt;
    float er = (ar * dt) * pos, ei = (ai * dt) * pos;
    float e = expf(er);
    float pr = e * cosf(ei), pi = e * sinf(ei);
    acc += Cr * pr - Ci * pi;
  }
  Kout[d * S + l] = 2.0f * acc;
}

__global__ void conv_naive(const float* Kk, const bf16* x, const bf16* dskip, bf16* b) {
  int d = blockIdx.y;
  int l = blockIdx.x * blockDim.x + threadIdx.x;
  float acc = 0.f;
  for (int m = 0; m <= l; ++m) acc += Kk[d * S + m] * b2f(x[(l - m) * D + d]);
  float y = rb(acc);
  float u = b2f(x[l * D + d]);
  b[l * D + d] = f2b(y + rb(u * b2f(dskip[d])));
}

__global__ void mix_naive(const bf16* x, const bf16* gw, const bf16* a, const bf16* b, bf16* out) {
  int row = blockIdx.x, t = threadIdx.x;
  __shared__ float red[256];
  float s = 0.f;
  for (int d = t; d < D; d += 256) s += rb(b2f(x[row * D + d]) * b2f(gw[d]));
  red[t] = s; __syncthreads();
  for (int o2 = 128; o2 > 0; o2 >>= 1) { if (t < o2) red[t] += red[t + o2]; __syncthreads(); }
  float sum = rb(red[0]);
  float g = rb(1.f / (1.f + expf(-sum)));
  float omg = rb(1.f - g);
  for (int d = t; d < D; d += 256) {
    float ga = rb(g * b2f(a[row * D + d]));
    float gb = rb(omg * b2f(b[row * D + d]));
    out[row * D + d] = f2b(ga + gb);
  }
}

extern "C" void kernel_launch(void* const* d_in, const int* in_sizes, int n_in,
                              void* d_out, int out_size, void* d_ws, size_t ws_size,
                              hipStream_t stream) {
  const bf16* x = (const bf16*)d_in[0];
  const bf16* wqkv = (const bf16*)d_in[1];
  const bf16* wo = (const bf16*)d_in[2];
  const bf16* lar = (const bf16*)d_in[4];
  const bf16* aim = (const bf16*)d_in[5];
  const bf16* cr = (const bf16*)d_in[6];
  const bf16* ci = (const bf16*)d_in[7];
  const bf16* ldt = (const bf16*)d_in[8];
  const bf16* dskip = (const bf16*)d_in[9];
  const bf16* gw = (const bf16*)d_in[10];
  bf16* out = (bf16*)d_out;
  char* ws = (char*)d_ws;
  bf16* qkv = (bf16*)ws;                       ws += (size_t)S * 3 * D * 2;
  bf16* o = (bf16*)ws;                         ws += (size_t)S * D * 2;
  bf16* a = (bf16*)ws;                         ws += (size_t)S * D * 2;
  bf16* b = (bf16*)ws;                         ws += (size_t)S * D * 2;
  float* Kk = (float*)ws;                      ws += (size_t)D * S * 4;
  gemm_naive<<<dim3((3 * D + 255) / 256, S), 256, 0, stream>>>(x, wqkv, qkv, S, 3 * D, D);
  attn_naive<<<dim3(H, S), 256, 0, stream>>>(qkv, o);
  gemm_naive<<<dim3((D + 255) / 256, S), 256, 0, stream>>>(o, wo, a, S, D, D);
  kgen_naive<<<dim3(S / 256, D), 256, 0, stream>>>(lar, aim, cr, ci, ldt, Kk);
  conv_naive<<<dim3(S / 256, D), 256, 0, stream>>>(Kk, x, dskip, b);
  mix_naive<<<S, 256, 0, stream>>>(x, gw, a, b, out);
}
```

```cpp
#include <hip/hip_runtime.h>
#include <hip/hip_bf16.h>

typedef __hip_bfloat16 bf16;
using bf16x8 = __attribute__((ext_vector_type(8))) short;
using s16x4 = __attribute__((ext_vector_type(4))) short;
using f32x16 = __attribute__((ext_vector_type(16))) float;
using u32x4 = __attribute__((ext_vector_type(4))) unsigned;
using u32x2 = __attribute__((ext_vector_type(2))) unsigned;

constexpr int S = 2048, D = 576, H = 32, DH = 18, N = 128;

__device__ __forceinline__ float b2f(unsigned short v) { return __uint_as_float(((unsigned)v) << 16); }
__device__ __forceinline__ unsigned short f2bu(float v) {
  bf16 b = __float2bfloat16(v);
  return *reinterpret_cast<unsigned short*>(&b);
}
__device__ __forceinline__ float rb(float v) { return b2f(f2bu(v)); }
__device__ __forceinline__ unsigned pack2(float lo, float hi) { return (unsigned)f2bu(lo) | ((unsigned)f2bu(hi) << 16); }

constexpr size_t WS_Q = 0;
constexpr size_t WS_K = WS_Q + (size_t)H * S * 32 * 2;
constexpr size_t WS_V = WS_K + (size_t)H * S * 32 * 2;
constexpr size_t WS_XT = WS_V + (size_t)H * S * 32 * 2;
constexpr size_t WS_G = WS_XT + (size_t)D * S * 2;
constexpr size_t WS_KF = WS_G + (size_t)S * 4;
constexpr size_t WS_O = WS_KF + (size_t)D * S * 4;
constexpr size_t WS_B = WS_O + (size_t)S * D * 2;
constexpr size_t WS_END = WS_B + (size_t)D * S * 2;

constexpr int BN = 64, BK = 64;
template <int BM>
__device__ __forceinline__ void gemm_tile(const unsigned short* __restrict__ A, const unsigned short* __restrict__ W,
                                          int lda, int ldw, int Kdim, int m0, int n0, char* smem,
                                          f32x16 (&acc)[BM / 64]) {
  constexpr int MT = BM / 64;
  constexpr int A_BYTES = BM * BK * 2;
  constexpr int B_BYTES = BK * BN * 2;
  constexpr int AV = A_BYTES / (256 * 16);
  constexpr int BV = B_BYTES / (256 * 16);
  const int tid = threadIdx.x, lane = tid & 63, w = tid >> 6, wm = w >> 1, wn = w & 1;
  const int r = lane & 31, hh = lane >> 5;
  char* sA = smem;
  char* sB = smem + 2 * A_BYTES;
  u32x4 ra[AV], rbv[BV];
  auto gload = [&](int kt) {
#pragma unroll
    for (int i = 0; i < AV; ++i) {
      int v = tid + i * 256, row = v >> 3, ch = v & 7;
      ra[i] = *reinterpret_cast<const u32x4*>(A + (size_t)(m0 + row) * lda + kt * BK + ch * 8);
    }
#pragma unroll
    for (int i = 0; i < BV; ++i) {
      int v = tid + i * 256, k = v >> 3, ch = v & 7;
      rbv[i] = *reinterpret_cast<const u32x4*>(W + (size_t)(kt * BK + k) * ldw + n0 + ch * 8);
    }
  };
  auto swrite = [&](int buf) {
#pragma unroll
    for (int i = 0; i < AV; ++i) {
      int v = tid + i * 256, row = v >> 3, ch = v & 7;
      *reinterpret_cast<u32x4*>(sA + buf * A_BYTES + row * 128 + ((ch ^ ((row >> 1) & 7)) << 4)) = ra[i];
    }
#pragma unroll
    for (int i = 0; i < BV; ++i) {
      int v = tid + i * 256, k = v >> 3, ch = v & 7;
      *reinterpret_cast<u32x4*>(sB + buf * B_BYTES + k * 128 + ((ch ^ (((k >> 1) & 1) << 2)) << 4)) = rbv[i];
    }
  };
#pragma unroll
  for (int i = 0; i < MT; ++i) acc[i] = f32x16{};
  const int nk = Kdim / BK;
  gload(0);
  swrite(0);
  __syncthreads();
  const int q4 = (lane & 15) >> 2, p4 = lane & 3, blk = (lane >> 4) & 1;
  for (int kt = 0; kt < nk; ++kt) {
    const int cur = kt & 1;
    if (kt + 1 < nk) gload(kt + 1);
    const char* a_base = sA + cur * A_BYTES;
    const char* b_base = sB + cur * B_BYTES;
#pragma unroll
    for (int kk = 0; kk < 4; ++kk) {
      s16x4 b0, b1;
      {
        int k0 = 16 * kk + 8 * hh + q4;
        int col = 32 * wn + 16 * blk + 4 * p4;
        int c0 = col ^ (((k0 >> 1) & 1) << 5);
        b0 = __builtin_amdgcn_ds_read_tr16_b64_v4i16(
            (__attribute__((address_space(3))) s16x4*)(uintptr_t)(unsigned)(uintptr_t)(b_base + k0 * 128 + c0 * 2));
        int k1 = k0 + 4;
        int c1 = col ^ (((k1 >> 1) & 1) << 5);
        b1 = __builtin_amdgcn_ds_read_tr16_b64_v4i16(
            (__attribute__((address_space(3))) s16x4*)(uintptr_t)(unsigned)(uintptr_t)(b_base + k1 * 128 + c1 * 2));
      }
      bf16x8 bfr = {b0[0], b0[1], b0[2], b0[3], b1[0], b1[1], b1[2], b1[3]};
#pragma unroll
      for (int mt = 0; mt < MT; ++mt) {
        int row = wm * (BM / 2) + mt * 32 + r;
        int ch = kk * 2 + hh;
        bf16x8 afr = *reinterpret_cast<const bf16x8*>(a_base + row * 128 + ((ch ^ ((row >> 1) & 7)) << 4));
        acc[mt] = __builtin_amdgcn_mfma_f32_32x32x16_bf16(afr, bfr, acc[mt], 0, 0, 0);
      }
    }
    if (kt + 1 < nk) swrite(cur ^ 1);
    __syncthreads();
  }
}
template <int BM>
constexpr int gemm_smem() { return 2 * (BM * BK * 2) + 2 * (BK * BN * 2); }

constexpr int QKV_BM = 128;
constexpr int QKV_NT = (3 * D) / BN;
constexpr int QKV_MT = S / QKV_BM;
constexpr int NB_QKV = QKV_NT * QKV_MT;
constexpr int NB_KGEN = D;
constexpr int NB_MISC = 64;
constexpr int NB_K1 = NB_KGEN + NB_QKV + NB_MISC;

__device__ __forceinline__ void kgen_block(int d, const unsigned short* __restrict__ lar, const unsigned short* __restrict__ aim,
                                           const unsigned short* __restrict__ cr, const unsigned short* __restrict__ ci,
                                           const unsigned short* __restrict__ ldt, float* __restrict__ Kf, char* smem) {
  float* cst = reinterpret_cast<float*>(smem);
  float* red = cst + 128 * 24;
  float* Kd = red + 4 * 10 * 64;
  const int tid = threadIdx.x, lane = tid & 63, w = tid >> 6;
  const float dt = rb(__expf(b2f(ldt[d])));
  if (tid < 128) {
    const int n = tid;
    const float ar = -rb(__expf(b2f(lar[d * N + n])));
    const float ai = b2f(aim[d * N + n]);
    const float adr = ar * dt, adi = ai * dt;
    const float Cr = b2f(cr[d * N + n]) * dt, Ci = b2f(ci[d * N + n]) * dt;
    const float offs[11] = {0.f, 1.f, 2.f, 3.f, 256.f, 258.f, 512.f, 516.f, 1024.f, 1032.f, 2048.f};
#pragma unroll
    for (int j = 0; j < 11; ++j) {
      float e = __expf(adr * offs[j]);
      float sn, cs;
      sincosf(adi * offs[j], &sn, &cs);
      float wr = e * cs, wi = e * sn;
      float Dr = 2.f * (Cr * wr - Ci * wi), Di = 2.f * (Cr * wi + Ci * wr);
      if (j < 10) { cst[n * 24 + 2 * j] = Dr; cst[n * 24 + 2 * j + 1] = Di; }
      else red[n] = Dr;
    }
    cst[n * 24 + 20] = adr * 1.4426950408889634f;
    cst[n * 24 + 21] = adi * 0.15915494309189535f;
  }
  __syncthreads();
  float k2048 = 0.f;
  if (w == 0) {
    k2048 = red[lane] + red[lane + 64];
    for (int o = 32; o > 0; o >>= 1) k2048 += __shfl_xor(k2048, o);
  }
  __syncthreads();
  float acc[10];
#pragma unroll
  for (int j = 0; j < 10; ++j) acc[j] = 0.f;
  const float pos0 = (float)(4 * lane);
  for (int nn = 0; nn < 32; ++nn) {
    const float* c = cst + (w * 32 + nn) * 24;
    float e = __builtin_amdgcn_exp2f(c[20] * pos0);
    float ph = c[21] * pos0;
    ph = ph - floorf(ph);
    float zr = e * __builtin_amdgcn_cosf(ph), zi = e * __builtin_amdgcn_sinf(ph);
    float z2r = zr * zr - zi * zi, z2i = 2.f * zr * zi;
    float z4r = z2r * z2r - z2i * z2i, z4i = 2.f * z2r * z2i;
#pragma unroll
    for (int j = 0; j < 6; ++j) acc[j] += c[2 * j] * zr - c[2 * j + 1] * zi;
#pragma unroll
    for (int j = 6; j < 8; ++j) acc[j] += c[2 * j] * z2r - c[2 * j + 1] * z2i;
#pragma unroll
    for (int j = 8; j < 10; ++j) acc[j] += c[2 * j] * z4r - c[2 * j + 1] * z4i;
  }
#pragma unroll
  for (int j = 0; j < 10; ++j) red[(w * 10 + j) * 64 + lane] = acc[j];
  __syncthreads();
  for (int i = tid; i < 640; i += 256) {
    int j = i >> 6, l = i & 63;
    float v = red[j * 64 + l] + red[(10 + j) * 64 + l] + red[(20 + j) * 64 + l] + red[(30 + j) * 64 + l];
    int pos;
    if (j < 4) pos = 4 * l + j;
    else if (j < 6) pos = 256 + 4 * l + 2 * (j - 4);
    else if (j < 8) pos = 512 + 8 * l + 4 * (j - 6);
    else pos = 1024 + 16 * l + 8 * (j - 8);
    Kd[pos] = v;
  }
  if (tid == 0) Kd[2048] = k2048;
  __syncthreads();
  for (int l = tid; l < S; l += 256) {
    int p = (int)rb((float)l);
    Kf[(size_t)d * S + l] = Kd[p];
  }
}

__global__ __launch_bounds__(256) void k1_kernel(const unsigned short* __restrict__ x, const unsigned short* __restrict__ wqkv,
                                                 const unsigned short* __restrict__ lar, const unsigned short* __restrict__ aim,
                                                 const unsigned short* __restrict__ cr, const unsigned short* __restrict__ ci,
                                                 const unsigned short* __restrict__ ldt, const unsigned short* __restrict__ gw,
                                                 char* __restrict__ ws) {
  extern __shared__ __attribute__((aligned(16))) char smem[];
  const int b = blockIdx.x;
  unsigned short* Qg = reinterpret_cast<unsigned short*>(ws + WS_Q);
  unsigned short* Kg = reinterpret_cast<unsigned short*>(ws + WS_K);
  unsigned short* Vg = reinterpret_cast<unsigned short*>(ws + WS_V);
  if (b < NB_KGEN) {
    kgen_block(b, lar, aim, cr, ci, ldt, reinterpret_cast<float*>(ws + WS_KF), smem);
    return;
  }
  if (b < NB_KGEN + NB_QKV) {
    const int t = b - NB_KGEN;
    const int mt = t / QKV_NT, nt = t % QKV_NT;
    f32x16 acc[QKV_BM / 64];
    gemm_tile<QKV_BM>(x, wqkv, D, 3 * D, D, mt * QKV_BM, nt * BN, smem, acc);
    const int lane = threadIdx.x & 63, w = threadIdx.x >> 6, wm = w >> 1, wn = w & 1, r = lane & 31, hh = lane >> 5;
    const int col = nt * BN + wn * 32 + r;
    const int which = col / D, c = col - which * D, head = c / DH, dim = c - head * DH;
#pragma unroll
    for (int m2 = 0; m2 < QKV_BM / 64; ++m2) {
#pragma unroll
      for (int g = 0; g < 4; ++g) {
        const int row0 = mt * QKV_BM + wm * (QKV_BM / 2) + m2 * 32 + 8 * g + 4 * hh;
        if (which == 0) {
#pragma unroll
          for (int i = 0; i < 4; ++i) Qg[((size_t)head * S + row0 + i) * 32 + dim] = f2bu(acc[m2][4 * g + i]);
        } else if (which == 1) {
#pragma unroll
          for (int i = 0; i < 4; ++i) {
            int key = row0 + i;
            int ch = (dim >> 3) ^ ((key >> 2) & 3);
            Kg[((size_t)head * S + key) * 32 + ch * 8 + (dim & 7)] = f2bu(acc[m2][4 * g + i]);
          }
        } else {
          int kb = row0 >> 6, k64 = row0 & 63;
          int sg = k64 >> 4, r16 = k64 & 15;
          int h2 = (r16 >> 2) & 1, j0 = 4 * (r16 >> 3);
          int ch = (2 * sg + h2) ^ ((dim >> 1) & 7);
          u32x2 pk = {pack2(acc[m2][4 * g], acc[m2][4 * g + 1]), pack2(acc[m2][4 * g + 2], acc[m2][4 * g + 3])};
          *reinterpret_cast<u32x2*>(Vg + (((size_t)head * 32 + kb) * 32 + dim) * 64 + ch * 8 + j0) = pk;
        }
      }
    }
    return;
  }
  const int mb = b - NB_KGEN - NB_QKV;
  const int tid = threadIdx.x;
  for (int i = mb * 256 + tid; i < H * S; i += NB_MISC * 256) {
    unsigned* qp = reinterpret_cast<unsigned*>(Qg + (size_t)i * 32 + 18);
#pragma unroll
    for (int j = 0; j < 7; ++j) qp[j] = 0u;
    int key = i & (S - 1);
    int sw = (key >> 2) & 3;
    unsigned* kp2 = reinterpret_cast<unsigned*>(Kg + (size_t)i * 32 + ((2 ^ sw) * 8) + 2);
    kp2[0] = 0u; kp2[1] = 0u; kp2[2] = 0u;
    u32x4 z = {0u, 0u, 0u, 0u};
    *reinterpret_cast<u32x4*>(Kg + (size_t)i * 32 + ((3 ^ sw) * 8)) = z;
  }
  for (int i = mb * 256 + tid; i < H * (S / 64) * 14 * 8; i += NB_MISC * 256) {
    int ch = i & 7, dd = (i >> 3) % 14, hb = i / (14 * 8);
    int dim = 18 + dd;
    unsigned v = (dim == 18) ? 0x3F803F80u : 0u;
    u32x4 z = {v, v, v, v};
    *reinterpret_cast<u32x4*>(Vg + ((size_t)hb * 32 + dim) * 64 + ch * 8) = z;
  }
  unsigned short* xT = reinterpret_cast<unsigned short*>(ws + WS_XT);
  {
    unsigned short* tile = reinterpret_cast<unsigned short*>(smem);
    for (int tl = mb; tl < (S / 64) * (D / 64); tl += NB_MISC) {
      int ts = tl / (D / 64), td = tl % (D / 64);
      __syncthreads();
      for (int i = tid; i < 64 * 64; i += 256) {
        int rr = i >> 6, cc = i & 63;
        tile[rr * 66 + cc] = x[(size_t)(ts * 64 + rr) * D + td * 64 + cc];
      }
      __syncthreads();
      for (int i = tid; i < 64 * 64; i += 256) {
        int rr = i >> 6, cc = i & 63;
        xT[(size_t)(td * 64 + rr) * S + ts * 64 + cc] = tile[cc * 66 + rr];
      }
    }
  }
  float* G = reinterpret_cast<float*>(ws + WS_G);
  {
    const int lane = tid & 63, w = tid >> 6;
    for (int row = mb * 4 + w; row < S; row += NB_MISC * 4) {
      float s = 0.f;
      for (int dd = lane; dd < D; dd += 64) s += rb(b2f(x[(size_t)row * D + dd]) * b2f(gw[dd]));
      for (int o = 32; o > 0; o >>= 1) s += __shfl_xor(s, o);
      if (lane == 0) {
        float sm = rb(s);
        G[row] = rb(1.f / (1.f + __expf(-sm)));
      }
    }
  }
}

constexpr int NB_ATT = H * (S / 128);
constexpr int NB_CONV = D;

__device__ __forceinline__ void attn_block(int head, int qb, const unsigned short* __restrict__ Qg, const unsigned short* __restrict__ Kg,
                                           const unsigned short* __restrict__ Vg, unsigned short* __restrict__ Og, char* smem) {
  const int tid = threadIdx.x, lane = tid & 63, w = tid >> 6, r = lane & 31, hh = lane >> 5;
  char* Ks = smem;
  char* Vs = smem + 8192;
  const int q = qb * 128 + w * 32 + r;
  bf16x8 qf0 = *reinterpret_cast<const bf16x8*>(Qg + ((size_t)head * S + q) * 32 + hh * 8);
  bf16x8 qf1 = *reinterpret_cast<const bf16x8*>(Qg + ((size_t)head * S + q) * 32 + 16 + hh * 8);
  const u32x4* Ksrc = reinterpret_cast<const u32x4*>(Kg + (size_t)head * S * 32);
  const u32x4* Vsrc = reinterpret_cast<const u32x4*>(Vg + (size_t)head * S * 32);
  constexpr float C = 0.23570226039551584f * 1.4426950408889634f;
  float m_run = -INFINITY;
  f32x16 o = {};
  u32x4 kr = Ksrc[tid], vr = Vsrc[tid];
  *reinterpret_cast<u32x4*>(Ks + tid * 16) = kr;
  *reinterpret_cast<u32x4*>(Vs + tid * 16) = vr;
  __syncthreads();
  constexpr int NT = S / 64;
  for (int t = 0; t < NT; ++t) {
    const int cur = t & 1;
    if (t + 1 < NT) { kr = Ksrc[(t + 1) * 256 + tid]; vr = Vsrc[(t + 1) * 256 + tid]; }
    const char* kb = Ks + cur * 4096;
    const char* vb = Vs + cur * 4096;
    f32x16 s0 = {}, s1 = {};
    {
      int key0 = r, key1 = 32 + r;
      bf16x8 k00 = *reinterpret_cast<const bf16x8*>(kb + key0 * 64 + (((0 + hh) ^ ((key0 >> 2) & 3)) << 4));
      bf16x8 k01 = *reinterpret_cast<const bf16x8*>(kb + key0 * 64 + (((2 + hh) ^ ((key0 >> 2) & 3)) << 4));
      bf16x8 k10 = *reinterpret_cast<const bf16x8*>(kb + key1 * 64 + (((0 + hh) ^ ((key1 >> 2) & 3)) << 4));
      bf16x8 k11 = *reinterpret_cast<const bf16x8*>(kb + key1 * 64 + (((2 + hh) ^ ((key1 >> 2) & 3)) << 4));
      s0 = __builtin_amdgcn_mfma_f32_32x32x16_bf16(k00, qf0, s0, 0, 0, 0);
      s0 = __builtin_amdgcn_mfma_f32_32x32x16_bf16(k01, qf1, s0, 0, 0, 0);
      s1 = __builtin_amdgcn_mfma_f32_32x32x16_bf16(k10, qf0, s1, 0, 0, 0);
      s1 = __builtin_amdgcn_mfma_f32_32x32x16_bf16(k11, qf1, s1, 0, 0, 0);
    }
    float tmax = s0[0];
#pragma unroll
    for (int i = 1; i < 16; ++i) tmax = fmaxf(tmax, s0[i]);
#pragma unroll
    for (int i = 0; i < 16; ++i) tmax = fmaxf(tmax, s1[i]);
    {
      auto rr = __builtin_amdgcn_permlane32_swap(__float_as_uint(tmax), __float_as_uint(tmax), false, false);
      tmax = fmaxf(__uint_as_float(rr[0]), __uint_as_float(rr[1]));
    }
    if (__any(tmax > m_run)) {
      float mn = fmaxf(m_run, tmax);
      float alpha = __builtin_amdgcn_exp2f((m_run - mn) * C);
#pragma unroll
      for (int i = 0; i < 16; ++i) o[i] *= alpha;
      m_run = mn;
    }
    const float mc = -m_run * C;
#pragma unroll
    for (int i = 0; i < 16; ++i) s0[i] = __builtin_amdgcn_exp2f(fmaf(s0[i], C, mc));
#pragma unroll
    for (int i = 0; i < 16; ++i) s1[i] = __builtin_amdgcn_exp2f(fmaf(s1[i], C, mc));
#pragma unroll
    for (int sg = 0; sg < 4; ++sg) {
      const f32x16& sx = (sg < 2) ? s0 : s1;
      const int base = (sg & 1) * 8;
      u32x4 pw = {pack2(sx[base + 0], sx[base + 1]), pack2(sx[base + 2], sx[base + 3]),
                  pack2(sx[base + 4], sx[base + 5]), pack2(sx[base + 6], sx[base + 7])};
      bf16x8 pf = *reinterpret_cast<bf16x8*>(&pw);
      bf16x8 vf = *reinterpret_cast<const bf16x8*>(vb + r * 128 + (((2 * sg + hh) ^ ((r >> 1) & 7)) << 4));
      o = __builtin_amdgcn_mfma_f32_32x32x16_bf16(vf, pf, o, 0, 0, 0);
    }
    if (t + 1 < NT) {
      *reinterpret_cast<u32x4*>(Ks + (cur ^ 1) * 4096 + tid * 16) = kr;
      *reinterpret_cast<u32x4*>(Vs + (cur ^ 1) * 4096 + tid * 16) = vr;
    }
    __syncthreads();
  }
  float l = __shfl(o[10], r);
  float inv = 1.f / l;
  unsigned short* op = Og + (size_t)q * D + head * DH;
#pragma unroll
  for (int g = 0; g < 3; ++g) {
    int dim0 = 8 * g + 4 * hh;
    if (dim0 < DH) {
      *reinterpret_cast<unsigned*>(op + dim0) = pack2(o[4 * g] * inv, o[4 * g + 1] * inv);
      if (dim0 + 2 < DH) *reinterpret_cast<unsigned*>(op + dim0 + 2) = pack2(o[4 * g + 2] * inv, o[4 * g + 3] * inv);
    }
  }
}

constexpr int KC_STRIDE = 2208;
constexpr int KC_T0 = 2047;
constexpr int U_STRIDE = 40;
__device__ __forceinline__ void conv_block(int d, const float* __restrict__ Kf, const unsigned short* __restrict__ xT,
                                           const unsigned short* __restrict__ dskip, unsigned short* __restrict__ Bc, char* smem) {
  const int tid = threadIdx.x, lane = tid & 63, w = tid >> 6, r = lane & 31, hh = lane >> 5;
  unsigned short* Kc = reinterpret_cast<unsigned short*>(smem);
  unsigned short* Us = Kc + 8 * KC_STRIDE;
  for (int i = tid; i < KC_STRIDE; i += 256) {
#pragma unroll
    for (int s = 0; s < 4; ++s) {
      int idx = KC_T0 - i - s;
      float kv = (idx >= 0 && idx < S) ? Kf[(size_t)d * S + idx] : 0.f;
      unsigned short hi = f2bu(kv);
      unsigned short lo = f2bu(kv - b2f(hi));
      Kc[s * KC_STRIDE + i] = hi;
      Kc[(4 + s) * KC_STRIDE + i] = lo;
    }
  }
  for (int i = tid; i < 65 * 32; i += 256) {
    int blk = i >> 5, e = i & 31;
    Us[blk * U_STRIDE + e] = (blk < 64) ? xT[(size_t)d * S + i] : (unsigned short)0;
  }
  __syncthreads();
  f32x16 acc0 = {}, acc1 = {};
  for (int k = w; k < 64; k += 4) {
#pragma unroll
    for (int kk = 0; kk < 2; ++kk) {
      int t0 = KC_T0 - (32 * k - 16 * kk - 8 * hh + r);
      int sc = t0 & 3, qq = t0 & ~3;
      const unsigned short* ph = Kc + sc * KC_STRIDE + qq;
      const unsigned short* pl = Kc + (4 + sc) * KC_STRIDE + qq;
      u32x2 h0 = *reinterpret_cast<const u32x2*>(ph), h1 = *reinterpret_cast<const u32x2*>(ph + 4);
      u32x2 l0 = *reinterpret_cast<const u32x2*>(pl), l1 = *reinterpret_cast<const u32x2*>(pl + 4);
      u32x4 hw = {h0[0], h0[1], h1[0], h1[1]}, lw = {l0[0], l0[1], l1[0], l1[1]};
      bf16x8 ah = *reinterpret_cast<bf16x8*>(&hw), al = *reinterpret_cast<bf16x8*>(&lw);
      int b1 = 32 + r - k; b1 = b1 < 0 ? 64 : b1;
      bf16x8 u1 = *reinterpret_cast<const bf16x8*>(Us + b1 * U_STRIDE + 16 * kk + 8 * hh);
      acc1 = __builtin_amdgcn_mfma_f32_32x32x16_bf16(ah, u1, acc1, 0, 0, 0);
      acc1 = __builtin_amdgcn_mfma_f32_32x32x16_bf16(al, u1, acc1, 0, 0, 0);
      if (k < 32) {
        int b0 = r - k; b0 = b0 < 0 ? 64 : b0;
        bf16x8 u0 = *reinterpret_cast<const bf16x8*>(Us + b0 * U_STRIDE + 16 * kk + 8 * hh);
        acc0 = __builtin_amdgcn_mfma_f32_32x32x16_bf16(ah, u0, acc0, 0, 0, 0);
        acc0 = __builtin_amdgcn_mfma_f32_32x32x16_bf16(al, u0, acc0, 0, 0, 0);
      }
    }
  }
  __syncthreads();
  float* red = reinterpret_cast<float*>(smem);
#pragma unroll
  for (int i = 0; i < 16; ++i) {
    int j = (i & 3) + 8 * (i >> 2) + 4 * hh;
    red[(w * 64 + r) * 33 + j] = acc0[i];
    red[(w * 64 + 32 + r) * 33 + j] = acc1[i];
  }
  __syncthreads();
  const float dsk = b2f(dskip[d]);
  for (int l = tid; l < S; l += 256) {
    int i = l >> 5, j = l & 31;
    float y = red[i * 33 + j] + red[(64 + i) * 33 + j] + red[(128 + i) * 33 + j] + red[(192 + i) * 33 + j];
    float u = b2f(xT[(size_t)d * S + l]);
    Bc[(size_t)d * S + l] = f2bu(rb(y) + rb(u * dsk));
  }
}

__global__ __launch_bounds__(256) void k2_kernel(const unsigned short* __restrict__ dskip, char* __restrict__ ws) {
  extern __shared__ __attribute__((aligned(16))) char smem[];
  const int b = blockIdx.x;
  if (b < NB_ATT) {
    attn_block(b >> 4, b & 15, reinterpret_cast<const unsigned short*>(ws + WS_Q), reinterpret_cast<const unsigned short*>(ws + WS_K),
               reinterpret_cast<const unsigned short*>(ws + WS_V), reinterpret_cast<unsigned short*>(ws + WS_O), smem);
  } else {
    conv_block(b - NB_ATT, reinterpret_cast<const float*>(ws + WS_KF), reinterpret_cast<const unsigned short*>(ws + WS_XT), dskip,
               reinterpret_cast<unsigned short*>(ws + WS_B), smem);
  }
}

constexpr int OP_BM = 64;
__global__ __launch_bounds__(256) void k3_kernel(const unsigned short* __restrict__ wo, const char* __restrict__ ws,
                                                 unsigned short* __restrict__ out) {
  extern __shared__ __attribute__((aligned(16))) char smem[];
  const int nt = blockIdx.x % (D / BN), mt = blockIdx.x / (D / BN);
  f32x16 acc[OP_BM / 64];
  gemm_tile<OP_BM>(reinterpret_cast<const unsigned short*>(ws + WS_O), wo, D, D, D, mt * OP_BM, nt * BN, smem, acc);
  const int lane = threadIdx.x & 63, w = threadIdx.x >> 6, wm = w >> 1, wn = w & 1, r = lane & 31, hh = lane >> 5;
  const int col = nt * BN + wn * 32 + r;
  const float* G = reinterpret_cast<const float*>(ws + WS_G);
  const unsigned short* Bc = reinterpret_cast<const unsigned short*>(ws + WS_B);
#pragma unroll
  for (int m2 = 0; m2 < OP_BM / 64; ++m2) {
#pragma unroll
    for (int g = 0; g < 4; ++g) {
      const int row0 = mt * OP_BM + wm * (OP_BM / 2) + m2 * 32 + 8 * g + 4 * hh;
      u32x2 bv = *reinterpret_cast<const u32x2*>(Bc + (size_t)col * S + row0);
#pragma unroll
      for (int i = 0; i < 4; ++i) {
        float gg = G[row0 + i];
        float a = rb(acc[m2][4 * g + i]);
        unsigned short bs = (unsigned short)((i & 1) ? (bv[i >> 1] >> 16) : (bv[i >> 1] & 0xffffu));
        float res = rb(gg * a) + rb(rb(1.f - gg) * b2f(bs));
        out[(size_t)(row0 + i) * D + col] = f2bu(res);
      }
    }
  }
}

extern "C" void kernel_launch(void* const* d_in, const int* in_sizes, int n_in,
                              void* d_out, int out_size, void* d_ws, size_t ws_size,
                              hipStream_t stream) {
  const unsigned short* x = (const unsigned short*)d_in[0];
  const unsigned short* wqkv = (const unsigned short*)d_in[1];
  const unsigned short* wo = (const unsigned short*)d_in[2];
  const unsigned short* lar = (const unsigned short*)d_in[4];
  const unsigned short* aim = (const unsigned short*)d_in[5];
  const unsigned short* cr = (const unsigned short*)d_in[6];
  const unsigned short* ci = (const unsigned short*)d_in[7];
  const unsigned short* ldt = (const unsigned short*)d_in[8];
  const unsigned short* dskip = (const unsigned short*)d_in[9];
  const unsigned short* gw = (const unsigned short*)d_in[10];
  char* ws = (char*)d_ws;
  constexpr int SM1 = gemm_smem<QKV_BM>();
  constexpr int SM2 = 8 * KC_STRIDE * 2 + 65 * U_STRIDE * 2;
  constexpr int SM3 = gemm_smem<OP_BM>();
  static_assert(SM1 >= (128 * 24 + 4 * 10 * 64 + 2052) * 4, "kgen smem");
  static_assert(SM2 >= 256 * 33 * 4 && SM2 >= 16384, "k2 smem");
  hipFuncSetAttribute((const void*)k1_kernel, hipFuncAttributeMaxDynamicSharedMemorySize, SM1);
  hipFuncSetAttribute((const void*)k2_kernel, hipFuncAttributeMaxDynamicSharedMemorySize, SM2);
  hipFuncSetAttribute((const void*)k3_kernel, hipFuncAttributeMaxDynamicSharedMemorySize, SM3);
  k1_kernel<<<NB_K1, 256, SM1, stream>>>(x, wqkv, lar, aim, cr, ci, ldt, gw, ws);
  k2_kernel<<<NB_ATT + NB_CONV, 256, SM2, stream>>>(dskip, ws);
  k3_kernel<<<(S / OP_BM) * (D / BN), 256, SM3, stream>>>(wo, ws, (unsigned short*)d_out);
}
```

```cpp
#include <hip/hip_runtime.h>
#include <hip/hip_bf16.h>

typedef __hip_bfloat16 bf16;
using bf16x8 = __attribute__((ext_vector_type(8))) short;
using s16x4 = __attribute__((ext_vector_type(4))) short;
using f32x16 = __attribute__((ext_vector_type(16))) float;
using u32x4 = __attribute__((ext_vector_type(4))) unsigned;
using u32x2 = __attribute__((ext_vector_type(2))) unsigned;

constexpr int S = 2048, D = 576, H = 32, DH = 18, N = 128;

__device__ __forceinline__ float b2f(unsigned short v) { return __uint_as_float(((unsigned)v) << 16); }
__device__ __forceinline__ unsigned short f2bu(float v) {
  bf16 b = __float2bfloat16(v);
  return *reinterpret_cast<unsigned short*>(&b);
}
__device__ __forceinline__ float rb(float v) { return b2f(f2bu(v)); }
__device__ __forceinline__ unsigned pack2(float lo, float hi) {
  unsigned r; asm("v_cvt_pk_bf16_f32 %0, %1, %2" : "=v"(r) : "v"(lo), "v"(hi)); return r;
}

constexpr size_t WS_Q = 0;
constexpr size_t WS_K = WS_Q + (size_t)H * S * 32 * 2;
constexpr size_t WS_V = WS_K + (size_t)H * S * 32 * 2;
constexpr size_t WS_XT = WS_V + (size_t)H * S * 32 * 2;
constexpr size_t WS_G = WS_XT + (size_t)D * S * 2;
constexpr size_t WS_KF = WS_G + (size_t)S * 4;
constexpr size_t WS_O = WS_KF + (size_t)2 * D * S * 4;
constexpr size_t WS_B = WS_O + (size_t)S * D * 2;
constexpr size_t WS_END = WS_B + (size_t)D * S * 2;

constexpr int BN = 64, BK = 64;
template <int BM>
__device__ __forceinline__ void gemm_tile(const unsigned short* __restrict__ A, const unsigned short* __restrict__ W,
                                          int lda, int ldw, int Kdim, int m0, int n0, char* smem,
                                          f32x16 (&acc)[BM / 64]) {
  constexpr int MT = BM / 64;
  constexpr int A_BYTES = BM * BK * 2;
  constexpr int B_BYTES = BK * BN * 2;
  constexpr int AV = A_BYTES / (256 * 16);
  constexpr int BV = B_BYTES / (256 * 16);
  const int tid = threadIdx.x, lane = tid & 63, w = tid >> 6, wm = w >> 1, wn = w & 1;
  const int r = lane & 31, hh = lane >> 5;
  char* sA = smem;
  char* sB = smem + 2 * A_BYTES;
  u32x4 ra[AV], rbv[BV];
  auto gload = [&](int kt) {
#pragma unroll
    for (int i = 0; i < AV; ++i) {
      int v = tid + i * 256, row = v >> 3, ch = v & 7;
      ra[i] = *reinterpret_cast<const u32x4*>(A + (size_t)(m0 + row) * lda + kt * BK + ch * 8);
    }
#pragma unroll
    for (int i = 0; i < BV; ++i) {
      int v = tid + i * 256, k = v >> 3, ch = v & 7;
      rbv[i] = *reinterpret_cast<const u32x4*>(W + (size_t)(kt * BK + k) * ldw + n0 + ch * 8);
    }
  };
  auto swrite = [&](int buf) {
#pragma unroll
    for (int i = 0; i < AV; ++i) {
      int v = tid + i * 256, row = v >> 3, ch = v & 7;
      *reinterpret_cast<u32x4*>(sA + buf * A_BYTES + row * 128 + ((ch ^ ((row >> 1) & 7)) << 4)) = ra[i];
    }
#pragma unroll
    for (int i = 0; i < BV; ++i) {
      int v = tid + i * 256, k = v >> 3, ch = v & 7;
      *reinterpret_cast<u32x4*>(sB + buf * B_BYTES + k * 128 + ((ch ^ (((k >> 1) & 1) << 2)) << 4)) = rbv[i];
    }
  };
#pragma unroll
  for (int i = 0; i < MT; ++i) acc[i] = f32x16{};
  const int nk = Kdim / BK;
  gload(0);
  swrite(0);
  __syncthreads();
  const int q4 = (lane & 15) >> 2, p4 = lane & 3, blk = (lane >> 4) & 1;
  for (int kt = 0; kt < nk; ++kt) {
    const int cur = kt & 1;
    if (kt + 1 < nk) gload(kt + 1);
    const char* a_base = sA + cur * A_BYTES;
    const char* b_base = sB + cur * B_BYTES;
#pragma unroll
    for (int kk = 0; kk < 4; ++kk) {
      s16x4 b0, b1;
      {
        int k0 = 16 * kk + 8 * hh + q4;
        int col = 32 * wn + 16 * blk + 4 * p4;
        int c0 = col ^ (((k0 >> 1) & 1) << 5);
        b0 = __builtin_amdgcn_ds_read_tr16_b64_v4i16(
            (__attribute__((address_space(3))) s16x4*)(uintptr_t)(unsigned)(uintptr_t)(b_base + k0 * 128 + c0 * 2));
        int k1 = k0 + 4;
        int c1 = col ^ (((k1 >> 1) & 1) << 5);
        b1 = __builtin_amdgcn_ds_read_tr16_b64_v4i16(
            (__attribute__((address_space(3))) s16x4*)(uintptr_t)(unsigned)(uintptr_t)(b_base + k1 * 128 + c1 * 2));
      }
      bf16x8 bfr = {b0[0], b0[1], b0[2], b0[3], b1[0], b1[1], b1[2], b1[3]};
#pragma unroll
      for (int mt = 0; mt < MT; ++mt) {
        int row = wm * (BM / 2) + mt * 32 + r;
        int ch = kk * 2 + hh;
        bf16x8 afr = *reinterpret_cast<const bf16x8*>(a_base + row * 128 + ((ch ^ ((row >> 1) & 7)) << 4));
        acc[mt] = __builtin_amdgcn_mfma_f32_32x32x16_bf16(afr, bfr, acc[mt], 0, 0, 0);
      }
    }
    if (kt + 1 < nk) swrite(cur ^ 1);
    __syncthreads();
  }
}
template <int BM>
constexpr int gemm_smem() { return 2 * (BM * BK * 2) + 2 * (BK * BN * 2); }

constexpr int QKV_BM = 64;
constexpr int QKV_NT = (3 * D) / BN;
constexpr int QKV_MT = S / QKV_BM;
constexpr int NB_QKV = QKV_NT * QKV_MT;
constexpr int NB_KGEN = 2 * D;
constexpr int NB_MISC = 288;
constexpr int NB_K1 = NB_KGEN + NB_QKV + NB_MISC;

__device__ __forceinline__ void kgen_block(int d, int nh, const unsigned short* __restrict__ lar, const unsigned short* __restrict__ aim,
                                           const unsigned short* __restrict__ cr, const unsigned short* __restrict__ ci,
                                           const unsigned short* __restrict__ ldt, float* __restrict__ Kf, char* smem) {
  float* cst = reinterpret_cast<float*>(smem);
  float* red = cst + 128 * 24;
  float* Kd = red + 4 * 10 * 64;
  const int tid = threadIdx.x, lane = tid & 63, w = tid >> 6;
  const float dt = rb(__expf(b2f(ldt[d])));
  if (tid < 64) {
    const int n = nh * 64 + tid;
    const float ar = -rb(__expf(b2f(lar[d * N + n])));
    const float ai = b2f(aim[d * N + n]);
    const float adr = ar * dt, adi = ai * dt;
    const float Cr = b2f(cr[d * N + n]) * dt, Ci = b2f(ci[d * N + n]) * dt;
    const float offs[11] = {0.f, 1.f, 2.f, 3.f, 256.f, 258.f, 512.f, 516.f, 1024.f, 1032.f, 2048.f};
#pragma unroll
    for (int j = 0; j < 11; ++j) {
      float e = __expf(adr * offs[j]);
      float ph = (adi * 0.15915494309189535f) * offs[j];
      ph = ph - floorf(ph);
      float sn = __builtin_amdgcn_sinf(ph), cs = __builtin_amdgcn_cosf(ph);
      float wr = e * cs, wi = e * sn;
      float Dr = 2.f * (Cr * wr - Ci * wi), Di = 2.f * (Cr * wi + Ci * wr);
      if (j < 10) { cst[tid * 24 + 2 * j] = Dr; cst[tid * 24 + 2 * j + 1] = Di; }
      else red[tid] = Dr;
    }
    cst[tid * 24 + 20] = adr * 1.4426950408889634f;
    cst[tid * 24 + 21] = adi * 0.15915494309189535f;
  }
  __syncthreads();
  float k2048 = 0.f;
  if (w == 0) {
    k2048 = red[lane];
    for (int o = 32; o > 0; o >>= 1) k2048 += __shfl_xor(k2048, o);
  }
  __syncthreads();
  float acc[10];
#pragma unroll
  for (int j = 0; j < 10; ++j) acc[j] = 0.f;
  const float pos0 = (float)(4 * lane);
  for (int nn = 0; nn < 16; ++nn) {
    const float* c = cst + (w * 16 + nn) * 24;
    float e = __builtin_amdgcn_exp2f(c[20] * pos0);
    float ph = c[21] * pos0;
    ph = ph - floorf(ph);
    float zr = e * __builtin_amdgcn_cosf(ph), zi = e * __builtin_amdgcn_sinf(ph);
    float z2r = zr * zr - zi * zi, z2i = 2.f * zr * zi;
    float z4r = z2r * z2r - z2i * z2i, z4i = 2.f * z2r * z2i;
#pragma unroll
    for (int j = 0; j < 6; ++j) acc[j] += c[2 * j] * zr - c[2 * j + 1] * zi;
#pragma unroll
    for (int j = 6; j < 8; ++j) acc[j] += c[2 * j] * z2r - c[2 * j + 1] * z2i;
#pragma unroll
    for (int j = 8; j < 10; ++j) acc[j] += c[2 * j] * z4r - c[2 * j + 1] * z4i;
  }
#pragma unroll
  for (int j = 0; j < 10; ++j) red[(w * 10 + j) * 64 + lane] = acc[j];
  __syncthreads();
  for (int i = tid; i < 640; i += 256) {
    int j = i >> 6, l = i & 63;
    float v = red[j * 64 + l] + red[(10 + j) * 64 + l] + red[(20 + j) * 64 + l] + red[(30 + j) * 64 + l];
    int pos;
    if (j < 4) pos = 4 * l + j;
    else if (j < 6) pos = 256 + 4 * l + 2 * (j - 4);
    else if (j < 8) pos = 512 + 8 * l + 4 * (j - 6);
    else pos = 1024 + 16 * l + 8 * (j - 8);
    Kd[pos] = v;
  }
  if (tid == 0) Kd[2048] = k2048;
  __syncthreads();
  for (int l = tid; l < S; l += 256) {
    int p = (int)rb((float)l);
    Kf[((size_t)nh * D + d) * S + l] = Kd[p];
  }
}

__global__ __launch_bounds__(256, 2) void k1_kernel(const unsigned short* __restrict__ x, const unsigned short* __restrict__ wqkv,
                                                 const unsigned short* __restrict__ lar, const unsigned short* __restrict__ aim,
                                                 const unsigned short* __restrict__ cr, const unsigned short* __restrict__ ci,
                                                 const unsigned short* __restrict__ ldt, const unsigned short* __restrict__ gw,
                                                 char* __restrict__ ws, int b0) {
  extern __shared__ __attribute__((aligned(16))) char smem[];
  static_assert(NB_KGEN == 4 * NB_MISC && NB_QKV == 3 * NB_MISC, "role interleave");
  int b = blockIdx.x + b0;
  if (b0 < 0) {
    const int g = blockIdx.x >> 3, j = blockIdx.x & 7;
    b = (j < 4) ? (g * 4 + j) : (j < 7) ? (NB_KGEN + g * 3 + (j - 4)) : (NB_KGEN + NB_QKV + g);
  }
  unsigned short* Qg = reinterpret_cast<unsigned short*>(ws + WS_Q);
  unsigned short* Kg = reinterpret_cast<unsigned short*>(ws + WS_K);
  unsigned short* Vg = reinterpret_cast<unsigned short*>(ws + WS_V);
  if (b < NB_KGEN) {
    kgen_block(b >> 1, b & 1, lar, aim, cr, ci, ldt, reinterpret_cast<float*>(ws + WS_KF), smem);
    return;
  }
  if (b < NB_KGEN + NB_QKV) {
    const int t = b - NB_KGEN;
    const int mt = t / QKV_NT, nt = t % QKV_NT;
    f32x16 acc[QKV_BM / 64];
    gemm_tile<QKV_BM>(x, wqkv, D, 3 * D, D, mt * QKV_BM, nt * BN, smem, acc);
    const int lane = threadIdx.x & 63, w = threadIdx.x >> 6, wm = w >> 1, wn = w & 1, r = lane & 31, hh = lane >> 5;
    const int col = nt * BN + wn * 32 + r;
    const int which = col / D, c = col - which * D, head = c / DH, dim = c - head * DH;
#pragma unroll
    for (int m2 = 0; m2 < QKV_BM / 64; ++m2) {
#pragma unroll
      for (int g = 0; g < 4; ++g) {
        const int row0 = mt * QKV_BM + wm * (QKV_BM / 2) + m2 * 32 + 8 * g + 4 * hh;
        if (which == 0) {
#pragma unroll
          for (int i = 0; i < 4; ++i) Qg[((size_t)head * S + row0 + i) * 32 + dim] = f2bu(acc[m2][4 * g + i] * (0.23570226039551584f * 1.4426950408889634f));
        } else if (which == 1) {
#pragma unroll
          for (int i = 0; i < 4; ++i) {
            int key = row0 + i;
            int ch = (dim >> 3) ^ ((key >> 2) & 3);
            Kg[((size_t)head * S + key) * 32 + ch * 8 + (dim & 7)] = f2bu(acc[m2][4 * g + i]);
          }
        } else {
          int kb = row0 >> 6, k64 = row0 & 63;
          int sg = k64 >> 4, r16 = k64 & 15;
          int h2 = (r16 >> 2) & 1, j0 = 4 * (r16 >> 3);
          int ch = (2 * sg + h2) ^ ((dim >> 1) & 7);
          u32x2 pk = {pack2(acc[m2][4 * g], acc[m2][4 * g + 1]), pack2(acc[m2][4 * g + 2], acc[m2][4 * g + 3])};
          *reinterpret_cast<u32x2*>(Vg + (((size_t)head * 32 + kb) * 32 + dim) * 64 + ch * 8 + j0) = pk;
        }
      }
    }
    return;
  }
  const int mb = b - NB_KGEN - NB_QKV;
  const int tid = threadIdx.x;
  for (int i = mb * 256 + tid; i < H * S; i += NB_MISC * 256) {
    unsigned* qp = reinterpret_cast<unsigned*>(Qg + (size_t)i * 32 + 18);
#pragma unroll
    for (int j = 0; j < 7; ++j) qp[j] = 0u;
    int key = i & (S - 1);
    int sw = (key >> 2) & 3;
    unsigned* kp2 = reinterpret_cast<unsigned*>(Kg + (size_t)i * 32 + ((2 ^ sw) * 8) + 2);
    kp2[0] = 0u; kp2[1] = 0u; kp2[2] = 0u;
    u32x4 z = {0u, 0u, 0u, 0u};
    *reinterpret_cast<u32x4*>(Kg + (size_t)i * 32 + ((3 ^ sw) * 8)) = z;
  }
  for (int i = mb * 256 + tid; i < H * (S / 64) * 14 * 8; i += NB_MISC * 256) {
    int ch = i & 7, dd = (i >> 3) % 14, hb = i / (14 * 8);
    int dim = 18 + dd;
    unsigned v = (dim == 18) ? 0x3F803F80u : 0u;
    u32x4 z = {v, v, v, v};
    *reinterpret_cast<u32x4*>(Vg + ((size_t)hb * 32 + dim) * 64 + ch * 8) = z;
  }
  unsigned short* xT = reinterpret_cast<unsigned short*>(ws + WS_XT);
  {
    unsigned short* tile = reinterpret_cast<unsigned short*>(smem);
    for (int tl = mb; tl < (S / 64) * (D / 64); tl += NB_MISC) {
      int ts = tl / (D / 64), td = tl % (D / 64);
      __syncthreads();
      for (int i = tid; i < 64 * 64; i += 256) {
        int rr = i >> 6, cc = i & 63;
        tile[rr * 66 + cc] = x[(size_t)(ts * 64 + rr) * D + td * 64 + cc];
      }
      __syncthreads();
      for (int i = tid; i < 64 * 64; i += 256) {
        int rr = i >> 6, cc = i & 63;
        xT[(size_t)(td * 64 + rr) * S + ts * 64 + cc] = tile[cc * 66 + rr];
      }
    }
  }
  float* G = reinterpret_cast<float*>(ws + WS_G);
  {
    const int lane = tid & 63, w = tid >> 6;
    for (int row = mb * 4 + w; row < S; row += NB_MISC * 4) {
      float s = 0.f;
      for (int dd = lane; dd < D; dd += 64) s += rb(b2f(x[(size_t)row * D + dd]) * b2f(gw[dd]));
      for (int o = 32; o > 0; o >>= 1) s += __shfl_xor(s, o);
      if (lane == 0) {
        float sm = rb(s);
        G[row] = rb(1.f / (1.f + __expf(-sm)));
      }
    }
  }
}

constexpr int NB_ATT = H * (S / 128);
constexpr float RESCALE_THR = 8.f;
constexpr int NB_CONV = D;

__device__ __forceinline__ void attn_block(int head, int qb, const unsigned short* __restrict__ Qg, const unsigned short* __restrict__ Kg,
                                           const unsigned short* __restrict__ Vg, unsigned short* __restrict__ Og, char* smem) {
  const int tid = threadIdx.x, lane = tid & 63, w = tid >> 6, r = lane & 31, hh = lane >> 5;
  const int qt = w & 3, kh = w >> 2;
  const int q = qb * 128 + qt * 32 + r;
  bf16x8 qf0 = *reinterpret_cast<const bf16x8*>(Qg + ((size_t)head * S + q) * 32 + hh * 8);
  bf16x8 qf1 = *reinterpret_cast<const bf16x8*>(Qg + ((size_t)head * S + q) * 32 + 16 + hh * 8);
  const int sh = tid >> 8, sidx = tid & 255;
  const u32x4* Ksrc = reinterpret_cast<const u32x4*>(Kg + (size_t)head * S * 32) + sh * (S / 2 * 4) + sidx;
  const u32x4* Vsrc = reinterpret_cast<const u32x4*>(Vg + (size_t)head * S * 32) + sh * (S / 2 * 4) + sidx;
  char* sdst = smem + sh * 8192 + sidx * 16;
  float m_run = -INFINITY;
  f32x16 o = {};
  f32x16 negm;
#pragma unroll
  for (int i = 0; i < 16; ++i) negm[i] = 0.f;
  float m_base = 0.f;
  u32x4 kr = Ksrc[0], vr = Vsrc[0];
  *reinterpret_cast<u32x4*>(sdst) = kr;
  *reinterpret_cast<u32x4*>(sdst + 4096) = vr;
  __syncthreads();
  constexpr int NT = S / 128;
  for (int t = 0; t < NT; ++t) {
    const int cur = t & 1;
    if (t + 1 < NT) { kr = Ksrc[(t + 1) * 256]; vr = Vsrc[(t + 1) * 256]; }
    const char* kb = smem + cur * 16384 + kh * 8192;
    const char* vb = kb + 4096;
    f32x16 s0, s1;
    {
      int key0 = r, key1 = 32 + r;
      bf16x8 k00 = *reinterpret_cast<const bf16x8*>(kb + key0 * 64 + (((0 + hh) ^ ((key0 >> 2) & 3)) << 4));
      bf16x8 k01 = *reinterpret_cast<const bf16x8*>(kb + key0 * 64 + (((2 + hh) ^ ((key0 >> 2) & 3)) << 4));
      bf16x8 k10 = *reinterpret_cast<const bf16x8*>(kb + key1 * 64 + (((0 + hh) ^ ((key1 >> 2) & 3)) << 4));
      bf16x8 k11 = *reinterpret_cast<const bf16x8*>(kb + key1 * 64 + (((2 + hh) ^ ((key1 >> 2) & 3)) << 4));
      s0 = __builtin_amdgcn_mfma_f32_32x32x16_bf16(k00, qf0, negm, 0, 0, 0);
      s0 = __builtin_amdgcn_mfma_f32_32x32x16_bf16(k01, qf1, s0, 0, 0, 0);
      s1 = __builtin_amdgcn_mfma_f32_32x32x16_bf16(k10, qf0, negm, 0, 0, 0);
      s1 = __builtin_amdgcn_mfma_f32_32x32x16_bf16(k11, qf1, s1, 0, 0, 0);
    }
    float tmax = fmaxf(fmaxf(s0[0], s0[1]), s0[2]);
#pragma unroll
    for (int i = 3; i < 15; i += 2) tmax = fmaxf(fmaxf(tmax, s0[i]), s0[i + 1]);
    tmax = fmaxf(tmax, s0[15]);
#pragma unroll
    for (int i = 0; i < 16; i += 2) tmax = fmaxf(fmaxf(tmax, s1[i]), s1[i + 1]);
    {
      auto rr = __builtin_amdgcn_permlane32_swap(__float_as_uint(tmax), __float_as_uint(tmax), false, false);
      tmax = fmaxf(__uint_as_float(rr[0]), __uint_as_float(rr[1]));
    }
    if (__any(m_base + tmax > m_run + RESCALE_THR)) {
      float mn = fmaxf(m_run, m_base + tmax);
      float alpha = __builtin_amdgcn_exp2f(m_run - mn);
      float delta = m_base - mn;
#pragma unroll
      for (int i = 0; i < 16; ++i) o[i] *= alpha;
#pragma unroll
      for (int i = 0; i < 16; ++i) { s0[i] += delta; s1[i] += delta; }
#pragma unroll
      for (int i = 0; i < 16; ++i) negm[i] = -mn;
      m_run = mn;
      m_base = mn;
    }
#pragma unroll
    for (int i = 0; i < 16; ++i) s0[i] = __builtin_amdgcn_exp2f(s0[i]);
#pragma unroll
    for (int i = 0; i < 16; ++i) s1[i] = __builtin_amdgcn_exp2f(s1[i]);
#pragma unroll
    for (int sg = 0; sg < 4; ++sg) {
      const f32x16& sx = (sg < 2) ? s0 : s1;
      const int base = (sg & 1) * 8;
      u32x4 pw = {pack2(sx[base + 0], sx[base + 1]), pack2(sx[base + 2], sx[base + 3]),
                  pack2(sx[base + 4], sx[base + 5]), pack2(sx[base + 6], sx[base + 7])};
      bf16x8 pf = *reinterpret_cast<bf16x8*>(&pw);
      bf16x8 vf = *reinterpret_cast<const bf16x8*>(vb + r * 128 + (((2 * sg + hh) ^ ((r >> 1) & 7)) << 4));
      o = __builtin_amdgcn_mfma_f32_32x32x16_bf16(vf, pf, o, 0, 0, 0);
    }
    if (t + 1 < NT) {
      *reinterpret_cast<u32x4*>(sdst + (cur ^ 1) * 16384) = kr;
      *reinterpret_cast<u32x4*>(sdst + (cur ^ 1) * 16384 + 4096) = vr;
    }
    __syncthreads();
  }
  float* mg = reinterpret_cast<float*>(smem) + qt * 17 * 64;
  if (kh == 1) {
    mg[lane] = m_run;
#pragma unroll
    for (int i = 0; i < 16; ++i) mg[(1 + i) * 64 + lane] = o[i];
  }
  __syncthreads();
  if (kh == 0) {
    float m1 = mg[lane];
    float mn = fmaxf(m_run, m1);
    float a0 = __builtin_amdgcn_exp2f(m_run - mn), a1 = __builtin_amdgcn_exp2f(m1 - mn);
#pragma unroll
    for (int i = 0; i < 16; ++i) o[i] = o[i] * a0 + mg[(1 + i) * 64 + lane] * a1;
    float l = __shfl(o[10], r);
    float inv = 1.f / l;
    unsigned short* op = Og + (size_t)q * D + head * DH;
#pragma unroll
    for (int g = 0; g < 3; ++g) {
      int dim0 = 8 * g + 4 * hh;
      if (dim0 < DH) {
        *reinterpret_cast<unsigned*>(op + dim0) = pack2(o[4 * g] * inv, o[4 * g + 1] * inv);
        if (dim0 + 2 < DH) *reinterpret_cast<unsigned*>(op + dim0 + 2) = pack2(o[4 * g + 2] * inv, o[4 * g + 3] * inv);
      }
    }
  }
}

constexpr int KC_STRIDE = 2208;
constexpr int KC_T0 = 2047;
constexpr int U_STRIDE = 40;
__device__ __forceinline__ void conv_block(int d, const float* __restrict__ Kf, const unsigned short* __restrict__ xT,
                                           const unsigned short* __restrict__ dskip, unsigned short* __restrict__ Bc, char* smem) {
  const int tid = threadIdx.x, lane = tid & 63, w = tid >> 6, r = lane & 31, hh = lane >> 5;
  unsigned short* Kc = reinterpret_cast<unsigned short*>(smem);
  unsigned short* Us = Kc + 8 * KC_STRIDE;
  {
    const float4 k0 = *reinterpret_cast<const float4*>(Kf + (size_t)d * S + tid * 4);
    const float4 k1 = *reinterpret_cast<const float4*>(Kf + ((size_t)D + d) * S + tid * 4);
    const float kv4[4] = {k0.x + k1.x, k0.y + k1.y, k0.z + k1.z, k0.w + k1.w};
#pragma unroll
    for (int e = 0; e < 4; ++e) {
      const int t = KC_T0 - (tid * 4 + e);
      const unsigned short hi = f2bu(kv4[e]);
      const unsigned short lo = f2bu(kv4[e] - b2f(hi));
#pragma unroll
      for (int s = 0; s < 4; ++s) {
        if (t - s >= 0) { Kc[s * KC_STRIDE + t - s] = hi; Kc[(4 + s) * KC_STRIDE + t - s] = lo; }
      }
    }
    for (int i = 2044 + tid; i < KC_STRIDE; i += 512) {
#pragma unroll
      for (int s = 0; s < 4; ++s) {
        if (i >= 2048 - s) { Kc[s * KC_STRIDE + i] = 0; Kc[(4 + s) * KC_STRIDE + i] = 0; }
      }
    }
  }
  for (int i = tid; i < 65 * 32; i += 512) {
    int blk = i >> 5, e = i & 31;
    Us[blk * U_STRIDE + e] = (blk < 64) ? xT[(size_t)d * S + i] : (unsigned short)0;
  }
  __syncthreads();
  f32x16 acc0 = {}, acc1 = {};
  for (int k = w; k < 64; k += 8) {
#pragma unroll
    for (int kk = 0; kk < 2; ++kk) {
      int t0 = KC_T0 - (32 * k - 16 * kk - 8 * hh + r);
      int sc = t0 & 3, qq = t0 & ~3;
      const unsigned short* ph = Kc + sc * KC_STRIDE + qq;
      const unsigned short* pl = Kc + (4 + sc) * KC_STRIDE + qq;
      u32x2 h0 = *reinterpret_cast<const u32x2*>(ph), h1 = *reinterpret_cast<const u32x2*>(ph + 4);
      u32x2 l0 = *reinterpret_cast<const u32x2*>(pl), l1 = *reinterpret_cast<const u32x2*>(pl + 4);
      u32x4 hw = {h0[0], h0[1], h1[0], h1[1]}, lw = {l0[0], l0[1], l1[0], l1[1]};
      bf16x8 ah = *reinterpret_cast<bf16x8*>(&hw), al = *reinterpret_cast<bf16x8*>(&lw);
      int b1 = 32 + r - k; b1 = b1 < 0 ? 64 : b1;
      bf16x8 u1 = *reinterpret_cast<const bf16x8*>(Us + b1 * U_STRIDE + 16 * kk + 8 * hh);
      acc1 = __builtin_amdgcn_mfma_f32_32x32x16_bf16(ah, u1, acc1, 0, 0, 0);
      acc1 = __builtin_amdgcn_mfma_f32_32x32x16_bf16(al, u1, acc1, 0, 0, 0);
      if (k < 32) {
        int b0 = r - k; b0 = b0 < 0 ? 64 : b0;
        bf16x8 u0 = *reinterpret_cast<const bf16x8*>(Us + b0 * U_STRIDE + 16 * kk + 8 * hh);
        acc0 = __builtin_amdgcn_mfma_f32_32x32x16_bf16(ah, u0, acc0, 0, 0, 0);
        acc0 = __builtin_amdgcn_mfma_f32_32x32x16_bf16(al, u0, acc0, 0, 0, 0);
      }
    }
  }
  __syncthreads();
  float* red = reinterpret_cast<float*>(smem);
#pragma unroll
  for (int i = 0; i < 16; ++i) {
    int j = (i & 3) + 8 * (i >> 2) + 4 * hh;
    red[(w * 64 + r) * 33 + j] = acc0[i];
    red[(w * 64 + 32 + r) * 33 + j] = acc1[i];
  }
  __syncthreads();
  const float dsk = b2f(dskip[d]);
  for (int l = tid; l < S; l += 512) {
    int i = l >> 5, j = l & 31;
    float y = 0.f;
#pragma unroll
    for (int ww = 0; ww < 8; ++ww) y += red[(ww * 64 + i) * 33 + j];
    float u = b2f(xT[(size_t)d * S + l]);
    Bc[(size_t)d * S + l] = f2bu(rb(y) + rb(u * dsk));
  }
}

__global__ __launch_bounds__(512, 4) void k2_kernel(const unsigned short* __restrict__ dskip, char* __restrict__ ws, int b0) {
  extern __shared__ __attribute__((aligned(16))) char smem[];
  const int b = blockIdx.x + b0;
  if (b < NB_ATT) {
    attn_block((b & 7) * 4 + (b >> 7), (b >> 3) & 15, reinterpret_cast<const unsigned short*>(ws + WS_Q), reinterpret_cast<const unsigned short*>(ws + WS_K),
               reinterpret_cast<const unsigned short*>(ws + WS_V), reinterpret_cast<unsigned short*>(ws + WS_O), smem);
  } else {
    conv_block(b - NB_ATT, reinterpret_cast<const float*>(ws + WS_KF), reinterpret_cast<const unsigned short*>(ws + WS_XT), dskip,
               reinterpret_cast<unsigned short*>(ws + WS_B), smem);
  }
}

constexpr int OP_BM = 64;
__global__ __launch_bounds__(256, 2) void k3_kernel(const unsigned short* __restrict__ wo, const char* __restrict__ ws,
                                                 unsigned short* __restrict__ out) {
  extern __shared__ __attribute__((aligned(16))) char smem[];
  const int nt = blockIdx.x % (D / BN), mt = blockIdx.x / (D / BN);
  f32x16 acc[OP_BM / 64];
  gemm_tile<OP_BM>(reinterpret_cast<const unsigned short*>(ws + WS_O), wo, D, D, D, mt * OP_BM, nt * BN, smem, acc);
  const int lane = threadIdx.x & 63, w = threadIdx.x >> 6, wm = w >> 1, wn = w & 1, r = lane & 31, hh = lane >> 5;
  const int col = nt * BN + wn * 32 + r;
  const float* G = reinterpret_cast<const float*>(ws + WS_G);
  const unsigned short* Bc = reinterpret_cast<const unsigned short*>(ws + WS_B);
#pragma unroll
  for (int m2 = 0; m2 < OP_BM / 64; ++m2) {
#pragma unroll
    for (int g = 0; g < 4; ++g) {
      const int row0 = mt * OP_BM + wm * (OP_BM / 2) + m2 * 32 + 8 * g + 4 * hh;
      u32x2 bv = *reinterpret_cast<const u32x2*>(Bc + (size_t)col * S + row0);
#pragma unroll
      for (int i = 0; i < 4; ++i) {
        float gg = G[row0 + i];
        float a = rb(acc[m2][4 * g + i]);
        unsigned short bs = (unsigned short)((i & 1) ? (bv[i >> 1] >> 16) : (bv[i >> 1] & 0xffffu));
        float res = rb(gg * a) + rb(rb(1.f - gg) * b2f(bs));
        out[(size_t)(row0 + i) * D + col] = f2bu(res);
      }
    }
  }
}

extern "C" void kernel_launch(void* const* d_in, const int* in_sizes, int n_in,
                              void* d_out, int out_size, void* d_ws, size_t ws_size,
                              hipStream_t stream) {
  const unsigned short* x = (const unsigned short*)d_in[0];
  const unsigned short* wqkv = (const unsigned short*)d_in[1];
  const unsigned short* wo = (const unsigned short*)d_in[2];
  const unsigned short* lar = (const unsigned short*)d_in[4];
  const unsigned short* aim = (const unsigned short*)d_in[5];
  const unsigned short* cr = (const unsigned short*)d_in[6];
  const unsigned short* ci = (const unsigned short*)d_in[7];
  const unsigned short* ldt = (const unsigned short*)d_in[8];
  const unsigned short* dskip = (const unsigned short*)d_in[9];
  const unsigned short* gw = (const unsigned short*)d_in[10];
  char* ws = (char*)d_ws;
  constexpr int SM1 = gemm_smem<QKV_BM>();
  constexpr int SM2 = 512 * 33 * 4;
  constexpr int SM3 = gemm_smem<OP_BM>();
  static_assert(SM1 >= (128 * 24 + 4 * 10 * 64 + 2052) * 4, "kgen smem");
  static_assert(SM2 >= 8 * KC_STRIDE * 2 + 65 * U_STRIDE * 2 && SM2 >= 32768, "k2 smem");
  hipFuncSetAttribute((const void*)k1_kernel, hipFuncAttributeMaxDynamicSharedMemorySize, SM1);
  hipFuncSetAttribute((const void*)k2_kernel, hipFuncAttributeMaxDynamicSharedMemorySize, SM2);
  hipFuncSetAttribute((const void*)k3_kernel, hipFuncAttributeMaxDynamicSharedMemorySize, SM3);
  k1_kernel<<<NB_K1, 256, SM1, stream>>>(x, wqkv, lar, aim, cr, ci, ldt, gw, ws, 0);
  k2_kernel<<<NB_ATT + NB_CONV, 512, SM2, stream>>>(dskip, ws, 0);
  k3_kernel<<<(S / OP_BM) * (D / BN), 256, SM3, stream>>>(wo, ws, (unsigned short*)d_out);
}
```
